```python
import torch

def _softmax(x, axis=-1):
    return torch.softmax(x, dim=axis)
def _gelu(x):
    return torch.nn.functional.gelu(x, approximate="tanh")
def _silu(x):
    return torch.nn.functional.silu(x)
def _sigmoid(x):
    return torch.sigmoid(x)
def _layernorm(x, axis=-1, eps=1e-5):
    m = x.mean(dim=axis, keepdim=True)
    v = x.var(dim=axis, keepdim=True, unbiased=False)
    return (x - m) / torch.sqrt(v + eps)
def _scatter_add(tgt, idx, src, axis=0):
    return tgt.clone().index_add_(axis, idx, src)
def _segment_sum(src, seg, n):
    out = torch.zeros((n,) + src.shape[1:], dtype=src.dtype)
    return out.index_add_(0, seg, src)
def _pad(x, pw):
    flat = tuple(v for pair in reversed(pw) for v in pair)
    return torch.nn.functional.pad(x, flat)

kernel_name = 'sequential_preLN__mha_block_cached__dense_activation_squared_relu__decode'

def setup_inputs():

    _w = (lambda a: (lambda t: t.to(torch.bfloat16) if t.is_floating_point() else t)(torch.as_tensor(a)))
    def _A_setup():
        B, Sq, D = 1, 1, 448
        H, Sk, Dh = 16, 32768, 256
        return {
            "x": _w((lambda *s: torch.randn(*s, dtype=torch.bfloat16) * 0.05)(B, Sq, D)),
            "k_cache": _w((lambda *s: torch.randn(*s, dtype=torch.bfloat16) * 3.2)(B, H, Sk, Dh)),
            "v_cache": _w((lambda *s: torch.randn(*s, dtype=torch.bfloat16) * 0.4)(B, H, Sk, Dh)),
            "wq": _w((lambda *s: torch.randn(*s, dtype=torch.bfloat16) * 3.2)(D, H * Dh) * (D ** -0.5)),
            "wo": _w((lambda *s: torch.randn(*s, dtype=torch.bfloat16) * 0.4)(H * Dh, D) * ((H * Dh) ** -0.5)),
        }
    _A_in = _A_setup()

    def _B_setup():
        B, S, D, F = 1, 1, 448, 1792
        return {
            "x": _w((lambda *s: torch.randn(*s, dtype=torch.bfloat16) * 0.05)(B, S, D)),
            "w1": _w((lambda *s: torch.randn(*s, dtype=torch.bfloat16) * 0.05)(D, F)),
            "b1": _w((lambda *s: torch.randn(*s, dtype=torch.bfloat16) * 0.8)(F)),
            "w2": _w((lambda *s: torch.randn(*s, dtype=torch.bfloat16) * 0.05)(F, D)),
            "b2": _w((lambda *s: torch.randn(*s, dtype=torch.bfloat16) * 3.2)(D)),
        }
    _B_in = _B_setup()

    return {
        "A_x": _A_in["x"],
        "A_k_cache": _A_in["k_cache"],
        "A_v_cache": _A_in["v_cache"],
        "A_wq": _A_in["wq"],
        "A_wo": _A_in["wo"],
        "B_x": _B_in["x"],
        "B_w1": _B_in["w1"],
        "B_b1": _B_in["b1"],
        "B_w2": _B_in["w2"],
        "B_b2": _B_in["b2"],
    }

def reference(A_x, A_k_cache, A_v_cache, A_wq, A_wo, B_x, B_w1, B_b1, B_w2, B_b2):
    def _A_ref(x, k_cache, v_cache, wq, wo):
        B, Sq, D = x.shape
        H, Dh = 16, 256
        q = (lambda x, *d: x.permute(*d))((x @ wq).reshape(B, Sq, H, Dh), 0, 2, 1, 3)
        scores = torch.einsum("bhqd,bhkd->bhqk", q, k_cache) * (Dh ** -0.5)
        p = _softmax(scores, axis=-1)
        o = torch.einsum("bhqk,bhkd->bhqd", p, v_cache)
        return (lambda x, *d: x.permute(*d))(o, 0, 2, 1, 3).reshape(B, Sq, H * Dh) @ wo

    def _B_ref(x, w1, b1, w2, b2):
        h = torch.einsum("bsd,df->bsf", x, w1) + b1
        zero = torch.zeros_like(h)
        r = torch.where(h > zero, h, zero)
        h = r * r
        out = torch.einsum("bsf,fd->bsd", h, w2) + b2
        return out

    x = A_x
    h = x + _A_ref(_layernorm(x, axis=-1), A_k_cache, A_v_cache, A_wq, A_wo)
    h2 = h + _B_ref(_layernorm(h, axis=-1), B_w1, B_b1, B_w2, B_b2)
    return h2
```

```cpp
#include <hip/hip_runtime.h>
#include <hip/hip_bf16.h>
#include <stdint.h>

#define MIMIC 0

#define GAS __attribute__((address_space(1)))
typedef unsigned u32x4 __attribute__((ext_vector_type(4)));
typedef float f32x4 __attribute__((ext_vector_type(4)));
typedef __bf16 bf16x2 __attribute__((ext_vector_type(2)));
typedef unsigned short u16;

constexpr int DM = 448, NH = 16, SK = 32768, DH = 256, FF = 1792;
constexpr int NSPLIT = 16, NWAVE = 8, NTHR = 512;
constexpr int KEYS_WG = SK / NSPLIT;
constexpr int KEYS_WAVE = KEYS_WG / NWAVE;
constexpr float EPS = 1e-5f;

__device__ __forceinline__ float bf2f(u16 b) { return __builtin_bit_cast(float, (unsigned)b << 16); }
__device__ __forceinline__ float lo16(unsigned u) { return __builtin_bit_cast(float, u << 16); }
__device__ __forceinline__ float hi16(unsigned u) { return __builtin_bit_cast(float, u & 0xffff0000u); }
__device__ __forceinline__ float rbf(float f) { return (float)(__bf16)f; }
__device__ __forceinline__ unsigned pack_bf(float a, float b) {
  bf16x2 v = {(__bf16)a, (__bf16)b};
  return __builtin_bit_cast(unsigned, v);
}
__device__ __forceinline__ float dot2(unsigned k, unsigned q, float acc) {
  return __builtin_amdgcn_fdot2_f32_bf16(__builtin_bit_cast(bf16x2, k), __builtin_bit_cast(bf16x2, q), acc, false);
}
template <int CTRL> __device__ __forceinline__ float dpp(float x) {
  return __builtin_bit_cast(float, __builtin_amdgcn_mov_dpp(__builtin_bit_cast(int, x), CTRL, 0xf, 0xf, true));
}
constexpr int XOR1 = 0xB1, XOR2 = 0x4E, XOR7 = 0x141, XOR8 = 0x128;
__device__ __forceinline__ float wave_sum(float x) {
  x += dpp<XOR1>(x); x += dpp<XOR2>(x); x += dpp<XOR7>(x); x += dpp<XOR8>(x);
  auto s = __builtin_amdgcn_permlane16_swap(__float_as_uint(x), __float_as_uint(x), false, false);
  x = __uint_as_float(s[0]) + __uint_as_float(s[1]);
  auto t = __builtin_amdgcn_permlane32_swap(__float_as_uint(x), __float_as_uint(x), false, false);
  return __uint_as_float(t[0]) + __uint_as_float(t[1]);
}
__device__ __forceinline__ float wave_max(float x) {
  x = fmaxf(x, dpp<XOR1>(x)); x = fmaxf(x, dpp<XOR2>(x)); x = fmaxf(x, dpp<XOR7>(x)); x = fmaxf(x, dpp<XOR8>(x));
  auto s = __builtin_amdgcn_permlane16_swap(__float_as_uint(x), __float_as_uint(x), false, false);
  x = fmaxf(__uint_as_float(s[0]), __uint_as_float(s[1]));
  auto t = __builtin_amdgcn_permlane32_swap(__float_as_uint(x), __float_as_uint(x), false, false);
  return fmaxf(__uint_as_float(t[0]), __uint_as_float(t[1]));
}
__device__ __forceinline__ u32x4 ldnt(const GAS u32x4* p) { return __builtin_nontemporal_load(p); }

struct Params {
  const u16* x; const u16* k; const u16* v; const u16* wq; const u16* wo;
  const u16* w1; const u16* b1; const u16* w2; const u16* b2;
  u16* out; float* ws;
};

__device__ __forceinline__ float block_layernorm(float v, bool act, float* red, int tid) {
  const int lane = tid & 63, w = tid >> 6;
  float s = wave_sum(act ? v : 0.f);
  if (lane == 0) red[w] = s;
  __syncthreads();
  float tot = 0.f;
#pragma unroll
  for (int i = 0; i < NWAVE; ++i) tot += red[i];
  float m = tot * (1.f / DM);
  float d = act ? v - m : 0.f;
  float s2 = wave_sum(d * d);
  if (lane == 0) red[8 + w] = s2;
  __syncthreads();
  float tot2 = 0.f;
#pragma unroll
  for (int i = 0; i < NWAVE; ++i) tot2 += red[8 + i];
  float var = tot2 * (1.f / DM);
  return d / sqrtf(var + EPS);
}

constexpr int WIN = 16;

__global__ void __launch_bounds__(NTHR, 2) attn_split(Params p) {
  __shared__ __attribute__((aligned(16))) float lds[8192];
  float* xn = lds;
  float* red = lds + 448;
  float* qpart = lds + 512;
  unsigned* qpk = (unsigned*)(lds + 512 + 2048);
  float* pbuf = lds + 3072;
  float* mrg = lds + 5120;
  const int tid = threadIdx.x, lane = tid & 63, w = __builtin_amdgcn_readfirstlane(tid >> 6);
  const int half = lane >> 5, c = lane & 31;
  const int b = blockIdx.x;
  const int h = (b & 7) + 8 * ((b >> 3) & 1), s = b >> 4;

  const GAS u32x4* wqp = (const GAS u32x4*)(p.wq + (size_t)(w * 56 + half * 28) * (NH * DH) + h * DH + 8 * c);
  u32x4 wq[28];
#pragma unroll
  for (int j = 0; j < 28; ++j) wq[j] = wqp[(size_t)j * (NH * DH / 8)];
  float xv = tid < DM ? bf2f(p.x[tid]) : 0.f;
  const size_t base = ((size_t)h * SK + (size_t)s * KEYS_WG + (size_t)w * KEYS_WAVE) * DH;
  const GAS u32x4* kp = (const GAS u32x4*)(p.k + base) + lane;
  const GAS u32x4* vp = (const GAS u32x4*)(p.v + base) + lane;
  u32x4 buf[WIN];
#pragma unroll
  for (int j = 0; j < WIN; ++j) buf[j] = ldnt(kp + j * 64);
  __builtin_amdgcn_sched_barrier(0);

  float xnv = block_layernorm(xv, tid < DM, red, tid);
  if (tid < DM) xn[tid] = xnv;
  __syncthreads();
  {
    float acc[8];
#pragma unroll
    for (int k = 0; k < 8; ++k) acc[k] = 0.f;
    const f32x4* xr = (const f32x4*)(xn + w * 56 + half * 28);
#pragma unroll
    for (int j4 = 0; j4 < 7; ++j4) {
      f32x4 xx = xr[j4];
#pragma unroll
      for (int jj = 0; jj < 4; ++jj) {
        const u32x4 wv = wq[j4 * 4 + jj];
#pragma unroll
        for (int k = 0; k < 4; ++k) { acc[2 * k] = fmaf(xx[jj], lo16(wv[k]), acc[2 * k]); acc[2 * k + 1] = fmaf(xx[jj], hi16(wv[k]), acc[2 * k + 1]); }
      }
    }
#pragma unroll
    for (int k = 0; k < 8; ++k) {
      auto t = __builtin_amdgcn_permlane32_swap(__float_as_uint(acc[k]), __float_as_uint(acc[k]), false, false);
      acc[k] = __uint_as_float(t[0]) + __uint_as_float(t[1]);
    }
    if (half == 0) {
      *(f32x4*)(qpart + w * 256 + 8 * c) = f32x4{acc[0], acc[1], acc[2], acc[3]};
      *(f32x4*)(qpart + w * 256 + 8 * c + 4) = f32x4{acc[4], acc[5], acc[6], acc[7]};
    }
  }
  __syncthreads();
  if (tid < 128) {
    float a0 = 0.f, a1 = 0.f;
#pragma unroll
    for (int i = 0; i < NWAVE; ++i) { a0 += qpart[i * 256 + 2 * tid]; a1 += qpart[i * 256 + 2 * tid + 1]; }
    qpk[tid] = pack_bf(a0, a1);
  }
  __syncthreads();
  const u32x4 q = *(const u32x4*)(qpk + 4 * c);

  float sc[4];
#pragma unroll
  for (int g = 0; g < 4; ++g) {
    float r2[2];
#pragma unroll
    for (int u = 0; u < 2; ++u) {
      float x[16];
      const int it = g * 2 + u;
#pragma unroll
      for (int j = 0; j < 16; ++j) {
        const u32x4 kv = buf[j];
        if (it < 7) buf[j] = ldnt(kp + ((it + 1) * 16 + j) * 64); else buf[j] = ldnt(vp + j * 64);
        float d = dot2(kv[0], q[0], 0.f); d = dot2(kv[1], q[1], d); d = dot2(kv[2], q[2], d); d = dot2(kv[3], q[3], d);
        x[j] = d;
        __builtin_amdgcn_sched_barrier(0);
      }
#define TR_STEP(HALF, CTRL, BIT) _Pragma("unroll") for (int hh = 0; hh < (HALF); ++hh) { \
        const float lo_ = x[hh], hi_ = x[hh + (HALF)]; const float t1_ = lo_ + dpp<CTRL>(lo_), t2_ = hi_ + dpp<CTRL>(hi_); \
        x[hh] = (lane & (BIT)) ? t2_ : t1_; }
      TR_STEP(8, XOR8, 8) TR_STEP(4, XOR7, 4) TR_STEP(2, XOR2, 2) TR_STEP(1, XOR1, 1)
#undef TR_STEP
      r2[u] = x[0];
    }
    auto t = __builtin_amdgcn_permlane16_swap(__float_as_uint(r2[0]), __float_as_uint(r2[1]), false, false);
    sc[g] = __uint_as_float(t[0]) + __uint_as_float(t[1]);
  }
  constexpr float C2 = 0.0625f * 1.4426950408889634f;
  float m = wave_max(fmaxf(fmaxf(sc[0], sc[1]), fmaxf(sc[2], sc[3])));
  float l = 0.f;
  float* pw = pbuf + w * 256;
  {
    const int u = (lane >> 4) & 1, hf = lane >> 5, i = lane & 15;
#pragma unroll
    for (int g = 0; g < 4; ++g) {
      const float pv = __builtin_amdgcn_exp2f((sc[g] - m) * C2);
      l += pv;
      pw[hf * 128 + g * 32 + u * 16 + i] = pv;
    }
  }
  l = wave_sum(l);
  asm volatile("s_waitcnt lgkmcnt(0)" ::: "memory");
  float acc[8];
#pragma unroll
  for (int k = 0; k < 8; ++k) acc[k] = 0.f;
  const f32x4* pr = (const f32x4*)(pw + half * 128);
#pragma unroll
  for (int it = 0; it < 8; ++it) {
#pragma unroll
    for (int j4 = 0; j4 < 4; ++j4) {
      const f32x4 pp = pr[it * 4 + j4];
#pragma unroll
      for (int jj = 0; jj < 4; ++jj) {
        const int j = j4 * 4 + jj;
        const u32x4 vv = buf[j];
        if (it < 7) buf[j] = ldnt(vp + ((it + 1) * 16 + j) * 64);
#pragma unroll
        for (int k = 0; k < 4; ++k) { acc[2 * k] = fmaf(pp[jj], lo16(vv[k]), acc[2 * k]); acc[2 * k + 1] = fmaf(pp[jj], hi16(vv[k]), acc[2 * k + 1]); }
        __builtin_amdgcn_sched_barrier(0);
      }
    }
  }
#pragma unroll
  for (int k = 0; k < 8; ++k) {
    auto t = __builtin_amdgcn_permlane32_swap(__float_as_uint(acc[k]), __float_as_uint(acc[k]), false, false);
    acc[k] = __uint_as_float(t[0]) + __uint_as_float(t[1]);
  }
  float* rec = mrg + w * 264;
  if (half == 0) {
    *(f32x4*)(rec + 8 + 8 * c) = f32x4{acc[0], acc[1], acc[2], acc[3]};
    *(f32x4*)(rec + 8 + 8 * c + 4) = f32x4{acc[4], acc[5], acc[6], acc[7]};
  }
  if (lane == 0) { rec[0] = m; rec[1] = l; }
  __syncthreads();
  if (tid < 256) {
    float M = mrg[0];
#pragma unroll
    for (int i = 1; i < NWAVE; ++i) M = fmaxf(M, mrg[i * 264]);
    float L = 0.f, o = 0.f;
#pragma unroll
    for (int i = 0; i < NWAVE; ++i) {
      const float wt = __builtin_amdgcn_exp2f((mrg[i * 264] - M) * C2);
      L = fmaf(wt, mrg[i * 264 + 1], L); o = fmaf(wt, mrg[i * 264 + 8 + tid], o);
    }
    float* part = p.ws + (size_t)(h * NSPLIT + s) * 264;
    part[8 + tid] = o;
    if (tid == 0) { part[0] = M; part[1] = L; }
  }
}

__global__ void __launch_bounds__(NTHR) combine_wo(Params p) {
  __shared__ float o[256];
  __shared__ float red[8 * 448];
  const int tid = threadIdx.x, h = blockIdx.x, lane = tid & 63, w = tid >> 6;
  constexpr float C2 = 0.0625f * 1.4426950408889634f;
  const float* part = p.ws + (size_t)h * NSPLIT * 264;
  if (tid < 256) {
    float M = part[0];
    for (int i = 1; i < NSPLIT; ++i) M = fmaxf(M, part[i * 264]);
    float L = 0.f, a = 0.f;
    for (int i = 0; i < NSPLIT; ++i) {
      const float wt = __builtin_amdgcn_exp2f((part[i * 264] - M) * C2);
      L = fmaf(wt, part[i * 264 + 1], L); a = fmaf(wt, part[i * 264 + 8 + tid], a);
    }
    a = a / L;
    o[tid] = a;
  }
  __syncthreads();
  float acc[8] = {0, 0, 0, 0, 0, 0, 0, 0};
  if (lane < 56) {
    for (int r = 0; r < 32; ++r) {
      const int row = w * 32 + r;
      const u32x4 wv = *(const u32x4*)(p.wo + (size_t)(h * 256 + row) * DM + 8 * lane);
      const float ov = o[row];
      for (int k = 0; k < 4; ++k) { acc[2 * k] = fmaf(ov, lo16(wv[k]), acc[2 * k]); acc[2 * k + 1] = fmaf(ov, hi16(wv[k]), acc[2 * k + 1]); }
    }
    for (int k = 0; k < 8; ++k) red[w * 448 + 8 * lane + k] = acc[k];
  }
  __syncthreads();
  if (tid < DM) {
    float a = 0.f;
    for (int i = 0; i < 8; ++i) a += red[i * 448 + tid];
    p.ws[131072 + h * 448 + tid] = a;
  }
}

__global__ void __launch_bounds__(NTHR) mlp1(Params p) {
  __shared__ float hn[448];
  __shared__ float red[16];
  __shared__ float fpart[4 * 112];
  __shared__ float f[112];
  const int tid = threadIdx.x, b = blockIdx.x;
  float hv = 0.f;
  if (tid < DM) {
    float a = 0.f;
    for (int i = 0; i < 16; ++i) a += p.ws[131072 + i * 448 + tid];
    hv = bf2f(p.x[tid]) + a;
    if (b == 0) p.ws[140000 + tid] = hv;
  }
  float n = block_layernorm(hv, tid < DM, red, tid);
  if (tid < DM) hn[tid] = n;
  __syncthreads();
  if (tid < 448) {
    const int col = tid % 112, kq = tid / 112;
    float a = 0.f;
    for (int k = kq * 112; k < kq * 112 + 112; ++k) a = fmaf(hn[k], bf2f(p.w1[(size_t)k * FF + b * 112 + col]), a);
    fpart[kq * 112 + col] = a;
  }
  __syncthreads();
  if (tid < 112) {
    float a = fpart[tid] + fpart[112 + tid] + fpart[224 + tid] + fpart[336 + tid];
    a += bf2f(p.b1[b * 112 + tid]);
    a = a > 0.f ? a : 0.f;
    f[tid] = a * a;
  }
  __syncthreads();
  if (tid < DM) {
    float a = 0.f;
    for (int k = 0; k < 112; ++k) a = fmaf(f[k], bf2f(p.w2[(size_t)(b * 112 + k) * DM + tid]), a);
    p.ws[150000 + b * 448 + tid] = a;
  }
}
__global__ void __launch_bounds__(NTHR) mlp2(Params p) {
  const int tid = threadIdx.x;
  if (tid < DM) {
    float a = 0.f;
    for (int i = 0; i < 16; ++i) a += p.ws[150000 + i * 448 + tid];
    a = p.ws[140000 + tid] + a + bf2f(p.b2[tid]);
    const __bf16 r = (__bf16)a;
    p.out[tid] = __builtin_bit_cast(u16, r);
  }
}

extern "C" void kernel_launch(void* const* d_in, const int* in_sizes, int n_in,
                              void* d_out, int out_size, void* d_ws, size_t ws_size,
                              hipStream_t stream) {
  Params p;
  p.x = (const u16*)d_in[0]; p.k = (const u16*)d_in[1]; p.v = (const u16*)d_in[2];
  p.wq = (const u16*)d_in[3]; p.wo = (const u16*)d_in[4];
  p.w1 = (const u16*)d_in[6]; p.b1 = (const u16*)d_in[7]; p.w2 = (const u16*)d_in[8]; p.b2 = (const u16*)d_in[9];
  p.out = (u16*)d_out; p.ws = (float*)d_ws;
  attn_split<<<NH * NSPLIT, NTHR, 0, stream>>>(p);
  combine_wo<<<NH, NTHR, 0, stream>>>(p);
  mlp1<<<16, NTHR, 0, stream>>>(p);
  mlp2<<<1, NTHR, 0, stream>>>(p);
}
```

```cpp
#include <hip/hip_runtime.h>
#include <hip/hip_bf16.h>
#include <stdint.h>
#include <type_traits>

#define GAS __attribute__((address_space(1)))
#define LAS __attribute__((address_space(3)))
typedef unsigned u32x4 __attribute__((ext_vector_type(4)));
typedef unsigned u32x2 __attribute__((ext_vector_type(2)));
typedef float f32x4 __attribute__((ext_vector_type(4)));
typedef __bf16 bf16x2 __attribute__((ext_vector_type(2)));
typedef __bf16 bf16x8 __attribute__((ext_vector_type(8)));
typedef short s16x4 __attribute__((ext_vector_type(4)));
typedef unsigned short u16;
typedef unsigned long long u64;

namespace {

constexpr int DM = 448, NH = 16, SK = 32768, DH = 256, FF = 1792;
constexpr int NSPLIT = 16, NSW = 4, NTHR = 512, NWG = NH * NSPLIT;
constexpr float EPS = 1e-5f;
constexpr float C2 = 0.0625f * 1.4426950408889634f;
constexpr unsigned SENT = 0xFFFFFFFFu;
constexpr unsigned SPIN_CAP = 1u << 17;
constexpr int AUX_NT = 2, AUX_SC1 = 16;
constexpr int RSRC_W3 = 0x00020000;

constexpr int KD = 5;
constexpr int PRE = 36;
constexpr int LIMP = 8;
constexpr int BURST = 6;
constexpr int CATCH = 5;
constexpr int DLK = 46;
constexpr int DLV = 45;
constexpr int TDRAIN = 6400;
constexpr int KD2 = 12;
constexpr int QWAIT = 480;
constexpr int QSLEEP = 12, PSLEEP = 2;
static_assert(PRE <= 48 && PRE >= KD && KD <= KD2 && KD2 <= 15, "look-ahead fits the 16-slot ring");
constexpr int k_issued(int n) { return n < 0 ? PRE : (n + KD + 1 > PRE + BURST + n / CATCH ? n + KD + 1 : PRE + BURST + n / CATCH); }
static_assert(PRE + BURST <= 48, "the parked loads fit the three register banks");
static_assert(k_issued(47) == 48 + KD, "the lead is back to KD by the end of the third group");

constexpr unsigned STEP = 64u * 4096u;
constexpr int NG = 16;
constexpr unsigned HEAD_BYTES = (unsigned)SK * DH * 2;

constexpr int NA = 4;
constexpr int ACOLS = DM / NA;
constexpr int NB = 28;
constexpr int BCOLS = FF / NB;

constexpr int WS_EPOCH = 0;
constexpr int WS_FLAGS = 4096;
constexpr int WS_QG = 8192;
constexpr int WS_PML = WS_QG + NH * 4 * DH * 8;
constexpr int WS_PO = WS_PML + 4096;
constexpr int WS_Y = WS_PO + NWG * 1024;
constexpr int WS_OP = WS_Y + NH * DM * 4;
constexpr int WS_END = WS_OP + NB * DM * 4;

constexpr int L_W = 0;
constexpr int L_W2 = DM * BCOLS * 2;
constexpr int L_P = 2 * L_W2;
constexpr int L_REC = L_P + 8192;
constexpr int L_ML = L_REC + 4096;
constexpr int L_QP = L_ML + 128;
constexpr int L_QT = L_QP + 1024;
constexpr int L_QPK = L_QT + 128;
constexpr int L_E = L_QPK + 512;
constexpr int L_FIN = L_E + 128;
constexpr int L_OB = L_FIN + 1024;
constexpr int L_F = L_OB + 1024;
constexpr int L_BYTES = L_F + 128;
static_assert(L_BYTES <= 160 * 1024 && L_BYTES > 80 * 1024, "one workgroup per CU");

struct Params {
  const u16* x; const u16* k; const u16* v; const u16* wq; const u16* wo;
  const u16* w1; const u16* b1; const u16* w2; const u16* b2;
  u16* out; unsigned char* ws;
};

__device__ __forceinline__ float bf2f(unsigned b) { return __builtin_bit_cast(float, b << 16); }
__device__ __forceinline__ float lo16(unsigned u) { return __builtin_bit_cast(float, u << 16); }
__device__ __forceinline__ float hi16(unsigned u) { return __builtin_bit_cast(float, u & 0xffff0000u); }
__device__ __forceinline__ unsigned bfbits(float f) { return __builtin_bit_cast(u16, (__bf16)f); }
__device__ __forceinline__ float rbf(float f) { return (float)(__bf16)f; }
__device__ __forceinline__ float dot2(unsigned a, unsigned b, float acc) {
  return __builtin_amdgcn_fdot2_f32_bf16(__builtin_bit_cast(bf16x2, a), __builtin_bit_cast(bf16x2, b), acc, false);
}
__device__ __forceinline__ void axpy8(float (&acc)[8], u32x4 wv, unsigned slo) {
  const unsigned shi = slo << 16;
#pragma unroll
  for (int k = 0; k < 4; ++k) { acc[2 * k] = dot2(wv[k], slo, acc[2 * k]); acc[2 * k + 1] = dot2(wv[k], shi, acc[2 * k + 1]); }
}
template <int CTRL> __device__ __forceinline__ float dpp(float x) {
  return __builtin_bit_cast(float, __builtin_amdgcn_mov_dpp(__builtin_bit_cast(int, x), CTRL, 0xf, 0xf, true));
}
constexpr int XOR1 = 0xB1, XOR2 = 0x4E, XOR7 = 0x141, XOR8 = 0x128;
__device__ __forceinline__ float sum_x16(float x) {
  auto s = __builtin_amdgcn_permlane16_swap(__float_as_uint(x), __float_as_uint(x), false, false);
  return __uint_as_float(s[0]) + __uint_as_float(s[1]);
}
__device__ __forceinline__ float sum_x32(float x) {
  auto s = __builtin_amdgcn_permlane32_swap(__float_as_uint(x), __float_as_uint(x), false, false);
  return __uint_as_float(s[0]) + __uint_as_float(s[1]);
}
__device__ __forceinline__ float max_x16(float x) {
  auto s = __builtin_amdgcn_permlane16_swap(__float_as_uint(x), __float_as_uint(x), false, false);
  return fmaxf(__uint_as_float(s[0]), __uint_as_float(s[1]));
}
__device__ __forceinline__ float max_x32(float x) {
  auto s = __builtin_amdgcn_permlane32_swap(__float_as_uint(x), __float_as_uint(x), false, false);
  return fmaxf(__uint_as_float(s[0]), __uint_as_float(s[1]));
}
__device__ __forceinline__ float oct_sum(float x) { x += dpp<XOR1>(x); x += dpp<XOR2>(x); x += dpp<XOR7>(x); return x; }
__device__ __forceinline__ float oct_max(float x) { x = fmaxf(x, dpp<XOR1>(x)); x = fmaxf(x, dpp<XOR2>(x)); x = fmaxf(x, dpp<XOR7>(x)); return x; }
__device__ __forceinline__ float row_sum(float x) { x = oct_sum(x); return x + dpp<XOR8>(x); }
__device__ __forceinline__ float row_max(float x) { x = oct_max(x); return fmaxf(x, dpp<XOR8>(x)); }
__device__ __forceinline__ float wave_sum(float x) { return sum_x32(sum_x16(row_sum(x))); }
__device__ __forceinline__ float wave_max(float x) { return max_x32(max_x16(row_max(x))); }
__device__ __forceinline__ unsigned canon(float f) { const unsigned u = __float_as_uint(f); return u < 0xFFFFFFFEu ? u : 0xFFFFFFFEu; }
__device__ __forceinline__ unsigned umax3(unsigned a, unsigned b, unsigned c) { const unsigned m = a > b ? a : b; return m > c ? m : c; }
__device__ __forceinline__ void lds_wait() { asm volatile("s_waitcnt lgkmcnt(0)" ::: "memory"); }
__device__ __forceinline__ void wg_barrier() { asm volatile("s_waitcnt lgkmcnt(0)\n\ts_barrier" ::: "memory"); }

template <int AUX> __device__ __forceinline__ u32x4 bld(__amdgpu_buffer_rsrc_t r, unsigned voff, unsigned soff) {
  return __builtin_amdgcn_raw_buffer_load_b128(r, voff, soff, AUX);
}
__device__ __forceinline__ unsigned ld_sc1(__amdgpu_buffer_rsrc_t r, unsigned voff, unsigned soff) {
  return __builtin_amdgcn_raw_buffer_load_b32(r, voff, soff, AUX_SC1);
}
__device__ __forceinline__ void st_sc1(__amdgpu_buffer_rsrc_t r, unsigned v, unsigned voff) {
  __builtin_amdgcn_raw_buffer_store_b32(v, r, voff, 0, AUX_SC1);
}
__device__ __forceinline__ void glds16(const void* gsrc, unsigned lds_dst) {
  unsigned keep;
  asm volatile("s_mov_b32 %0, m0\n\ts_mov_b32 m0, %2\n\ts_nop 0\n\tglobal_load_lds_dwordx4 %1, off nt\n\ts_mov_b32 m0, %0"
               : "=&s"(keep) : "v"(gsrc), "s"(lds_dst) : "memory");
}
__device__ __forceinline__ bf16x8 bfrag(const LAS unsigned char* img, int stride, int k0, int n0, int lane) {
  const int row = k0 + 8 * (lane >> 4) + ((lane & 15) >> 2), col = n0 + 4 * (lane & 3);
  const LAS unsigned char* a = img + row * stride + col * 2;
  const s16x4 lo = __builtin_amdgcn_ds_read_tr16_b64_v4i16((LAS s16x4*)a);
  const s16x4 hi = __builtin_amdgcn_ds_read_tr16_b64_v4i16((LAS s16x4*)(a + 4 * stride));
  return __builtin_bit_cast(bf16x8, __builtin_shufflevector(lo, hi, 0, 1, 2, 3, 4, 5, 6, 7));
}
__device__ __forceinline__ f32x4 mfma(bf16x8 a, bf16x8 b, f32x4 c) { return __builtin_amdgcn_mfma_f32_16x16x32_bf16(a, b, c, 0, 0, 0); }

#define FMAC_BC(acc, wt, r, i) asm("v_fmac_f32_dpp %0, %1, %2 row_newbcast:" #i " row_mask:0xf bank_mask:0xf" : "+v"(acc) : "v"(wt), "v"(r))
__device__ __forceinline__ f32x4 bcast_fma4x4(float wt, f32x4 r0, f32x4 r1, f32x4 r2, f32x4 r3) {
  float a[4] = {0.f, 0.f, 0.f, 0.f};
  asm volatile("s_nop 1" : "+v"(wt));
#pragma unroll
  for (int k = 0; k < 4; ++k) {
    const float x0 = r0[k], x1 = r1[k], x2 = r2[k], x3 = r3[k];
    FMAC_BC(a[k], wt, x0, 0); FMAC_BC(a[k], wt, x1, 1); FMAC_BC(a[k], wt, x2, 2); FMAC_BC(a[k], wt, x3, 3);
  }
  return f32x4{a[0], a[1], a[2], a[3]};
}
__device__ __forceinline__ float bcast_fma16(float wt, const float (&r)[16]) {
  float a0 = 0.f, a1 = 0.f;
  asm volatile("s_nop 1" : "+v"(wt));
  FMAC_BC(a0, wt, r[0], 0); FMAC_BC(a1, wt, r[1], 1); FMAC_BC(a0, wt, r[2], 2); FMAC_BC(a1, wt, r[3], 3);
  FMAC_BC(a0, wt, r[4], 4); FMAC_BC(a1, wt, r[5], 5); FMAC_BC(a0, wt, r[6], 6); FMAC_BC(a1, wt, r[7], 7);
  FMAC_BC(a0, wt, r[8], 8); FMAC_BC(a1, wt, r[9], 9); FMAC_BC(a0, wt, r[10], 10); FMAC_BC(a1, wt, r[11], 11);
  FMAC_BC(a0, wt, r[12], 12); FMAC_BC(a1, wt, r[13], 13); FMAC_BC(a0, wt, r[14], 14); FMAC_BC(a1, wt, r[15], 15);
  return a0 + a1;
}

__device__ __forceinline__ float ln_rden(float var) { return __builtin_amdgcn_rcpf(rbf(__builtin_amdgcn_sqrtf(rbf(rbf(var) + EPS)))); }
__device__ __forceinline__ void ln_stats(u32x4 xr, float c0, bool act, float& mb, float& rden) {
  float d[8];
#pragma unroll
  for (int k = 0; k < 4; ++k) { d[2 * k] = lo16(xr[k]) - c0; d[2 * k + 1] = hi16(xr[k]) - c0; }
  float s1 = ((d[0] + d[1]) + (d[2] + d[3])) + ((d[4] + d[5]) + (d[6] + d[7]));
  float s2 = fmaf(d[0], d[0], fmaf(d[1], d[1], fmaf(d[2], d[2], d[3] * d[3]))) + fmaf(d[4], d[4], fmaf(d[5], d[5], fmaf(d[6], d[6], d[7] * d[7])));
  s1 = act ? s1 : 0.f; s2 = act ? s2 : 0.f;
  s1 += dpp<XOR1>(s1); s2 += dpp<XOR1>(s2);
  s1 += dpp<XOR2>(s1); s2 += dpp<XOR2>(s2);
  s1 += dpp<XOR7>(s1); s2 += dpp<XOR7>(s2);
  s1 += dpp<XOR8>(s1); s2 += dpp<XOR8>(s2);
  s1 = sum_x16(s1); s2 = sum_x16(s2);
  s1 = sum_x32(s1); s2 = sum_x32(s2);
  const float md = s1 * (1.f / DM);
  const float var = fmaxf(fmaf(-md, md, s2 * (1.f / DM)), 0.f);
  mb = rbf(c0 + md);
  rden = ln_rden(var);
}

template <int CTRL, int BIT> __device__ __forceinline__ float pair_reduce(float lo, float hi, int lane) {
  const float t1 = lo + dpp<CTRL>(lo), t2 = hi + dpp<CTRL>(hi);
  return (lane & BIT) ? t2 : t1;
}
#define TR_STEP(HALF, CTRL, BIT) _Pragma("unroll") for (int hh = 0; hh < (HALF); ++hh) { \
    const float lo_ = x[hh], hi_ = x[hh + (HALF)]; const float t1_ = lo_ + dpp<CTRL>(lo_), t2_ = hi_ + dpp<CTRL>(hi_); \
    x[hh] = (lane & (BIT)) ? t2_ : t1_; }

__global__ void __launch_bounds__(NTHR, 2) fused_decode_block(Params p) {
  extern __shared__ __attribute__((aligned(16))) unsigned char smem[];
  LAS unsigned char* L = (LAS unsigned char*)smem;
  const int tid = threadIdx.x, lane = tid & 63, w = __builtin_amdgcn_readfirstlane(tid >> 6);
  const int half = lane >> 5, c = lane & 31;
  const int b = blockIdx.x;
  const int h = (b & 7) + 8 * (b >> 7), s = (b >> 3) & 15;
  const int hq = 2 * (b & 7) + (b >> 7), sq = (b >> 3) & 15;
  const int bb = h * NSPLIT + s;
  const int jB = h * 2 + (s - NA);
  const bool roleA = s < NA, roleB = s >= NA && s < NA + 2 && jB < NB;

  const __amdgpu_buffer_rsrc_t rws = __builtin_amdgcn_make_buffer_rsrc((void*)p.ws, 0, WS_END, RSRC_W3);
  const unsigned vo = lane * 16;

  if (w >= NSW) {
    const int hw = w - NSW;
    const int cg = sq >> 2, kq = sq & 3;
    const unsigned t_entry = (unsigned)__builtin_amdgcn_s_memrealtime();
    const __amdgpu_buffer_rsrc_t rx = __builtin_amdgcn_make_buffer_rsrc((void*)p.x, 0, DM * 2, RSRC_W3);
    u32x4 xr = bld<0>(rx, vo, 0);
    u32x2 ep = __builtin_amdgcn_raw_buffer_load_b64(rws, WS_EPOCH, 0, AUX_SC1);
    unsigned xrow[4];
    u32x4 wq[4];
#pragma unroll
    for (int i = 0; i < 4; ++i) {
      const int row = 112 * kq + 28 * hw + ((i == 3 && half) ? 0 : 8 * i + (lane >> 3));
      wq[i] = *(const GAS u32x4*)(p.wq + (size_t)row * (NH * DH) + hq * DH + 64 * cg + 8 * (lane & 7));
    }
#pragma unroll
    for (int i = 0; i < 4; ++i) xrow[i] = p.x[112 * kq + 28 * hw + ((i == 3 && half) ? 0 : 8 * i + (lane >> 3))];
    if (lane == 0) *(volatile LAS unsigned*)(L + L_QT + hw * 4) = 0u;
    wg_barrier();
    asm volatile("" : "+v"(xr), "+v"(xrow[0]), "+v"(xrow[1]), "+v"(xrow[2]), "+v"(xrow[3]));
    {
      float mb, rden;
      ln_stats(xr, bf2f(__builtin_amdgcn_readfirstlane(xr[0]) & 0xffffu), lane < 56, mb, rden);
      float x[8];
#pragma unroll
      for (int k = 0; k < 8; ++k) x[k] = 0.f;
#pragma unroll
      for (int i = 0; i < 4; ++i) {
        unsigned nb = bfbits(rbf(bf2f(xrow[i]) - mb) * rden);
        if (i == 3) nb = half ? 0u : nb;
        axpy8(x, wq[i], nb);
      }
      TR_STEP(4, XOR8, 8)
#pragma unroll
      for (int k = 0; k < 2; ++k) {
        auto sw = __builtin_amdgcn_permlane16_swap(__float_as_uint(x[k]), __float_as_uint(x[k + 2]), false, false);
        x[k] = __uint_as_float(sw[0]) + __uint_as_float(sw[1]);
      }
      auto sw = __builtin_amdgcn_permlane32_swap(__float_as_uint(x[0]), __float_as_uint(x[1]), false, false);
      const float qs = __uint_as_float(sw[0]) + __uint_as_float(sw[1]);
      ((LAS float*)(L + L_QP))[hw * 64 + 8 * (lane & 7) + ((lane >> 1) & 4) + ((lane >> 3) & 2) + half] = qs;
      lds_wait();
      if (lane == 0) *(volatile LAS unsigned*)(L + L_QT + hw * 4) = 1u;
    }
    asm volatile("" : "+v"(ep));
    const u64 e1 = (((u64)ep[1] << 32) | ep[0]) + 1;
    const unsigned tlo = (unsigned)e1 << 27, thi = (unsigned)(e1 >> 5);
    if (hw == 0) {
      for (unsigned spins = 0; spins < SPIN_CAP; ++spins) {
        const unsigned t = *(volatile LAS unsigned*)(L + L_QT + (lane & 3) * 4);
        if (__all(t == 1u)) break;
      }
      asm volatile("" ::: "memory");
      const LAS float* qp = (const LAS float*)(L + L_QP) + lane;
      const unsigned u = __float_as_uint((qp[0] + qp[64]) + (qp[128] + qp[192]));
      const unsigned v27 = (u + ((u & 0x7f800000u) == 0x7f800000u ? 0u : 0x10u)) >> 5;
      __builtin_amdgcn_raw_buffer_store_b64(u32x2{tlo | v27, thi}, rws, WS_QG + ((hq * 4 + kq) * DH + 64 * cg + lane) * 8, 0, AUX_SC1);
    }
    while ((int)((unsigned)__builtin_amdgcn_s_memrealtime() - t_entry) < QWAIT) __builtin_amdgcn_s_sleep(4);
    u32x2 g[4];
    for (unsigned spins = 0;; ++spins) {
      asm volatile("" ::: "memory");
#pragma unroll
      for (int i = 0; i < 4; ++i) g[i] = __builtin_amdgcn_raw_buffer_load_b64(rws, WS_QG + (h * 4 * DH + hw * 64 + lane) * 8, i * DH * 8, AUX_SC1);
      __builtin_amdgcn_sched_barrier(0);
      const unsigned bad = (((g[0][0] ^ tlo) | (g[1][0] ^ tlo) | (g[2][0] ^ tlo) | (g[3][0] ^ tlo)) >> 27) |
                           (g[0][1] ^ thi) | (g[1][1] ^ thi) | (g[2][1] ^ thi) | (g[3][1] ^ thi);
      if (__all(bad == 0) || spins > SPIN_CAP) break;
      __builtin_amdgcn_s_sleep(QSLEEP);
    }
    const unsigned qbits = bfbits((__uint_as_float(g[0][0] << 5) + __uint_as_float(g[1][0] << 5)) + (__uint_as_float(g[2][0] << 5) + __uint_as_float(g[3][0] << 5)));
    ((LAS u16*)(L + L_QPK))[hw * 64 + lane] = (u16)qbits;
    if (hw == 0 && lane == 0) *(LAS u32x2*)(L + L_E) = u32x2{(unsigned)e1, (unsigned)(e1 >> 32)};
    wg_barrier();
    if (hw == 0) {
      const u32x4 sv = {SENT, SENT, SENT, SENT};
      __builtin_amdgcn_raw_buffer_store_b128(sv, rws, WS_PO + bb * 1024 + vo, 0, AUX_SC1);
      if (lane == 0) __builtin_amdgcn_raw_buffer_store_b64(u32x2{SENT, SENT}, rws, WS_PML + bb * 8, 0, AUX_SC1);
      if (roleA && lane < ACOLS / 4) __builtin_amdgcn_raw_buffer_store_b128(sv, rws, WS_Y + (h * DM + s * ACOLS) * 4 + vo, 0, AUX_SC1);
      if (roleB) {
        __builtin_amdgcn_raw_buffer_store_b128(sv, rws, WS_OP + jB * DM * 4 + vo, 0, AUX_SC1);
        if (lane < 48) __builtin_amdgcn_raw_buffer_store_b128(sv, rws, WS_OP + jB * DM * 4 + 1024 + vo, 0, AUX_SC1);
      }
      asm volatile("s_waitcnt vmcnt(0)" ::: "memory");
      if (lane == 0) __builtin_amdgcn_raw_buffer_store_b64(u32x2{(unsigned)e1, (unsigned)(e1 >> 32)}, rws, WS_FLAGS + b * 8, 0, AUX_SC1);
    }
    const unsigned lbase = (unsigned)(uintptr_t)L;
    {
      constexpr int NDW = 4;
      if (roleA) {
        for (int n = hw; n < 56; n += NDW) {
          const int ch = 64 * n + lane, row = ch / 14, cc = ch - row * 14;
          glds16(p.wo + (size_t)(h * DH + row) * DM + s * ACOLS + 8 * cc, lbase + L_W + n * 1024);
          if ((n / NDW) & 1) asm volatile("s_waitcnt vmcnt(0)" ::: "memory");
        }
      } else if (roleB) {
        for (int n = hw; n < 56; n += NDW) {
          const int ch = 64 * n + lane;
          glds16(p.w1 + (size_t)(ch >> 3) * FF + jB * BCOLS + 8 * (ch & 7), lbase + L_W + n * 1024);
          glds16(p.w2 + (size_t)jB * BCOLS * DM + (size_t)ch * 8, lbase + L_W2 + n * 1024);
          asm volatile("s_waitcnt vmcnt(0)" ::: "memory");
        }
      }
    }
    if (hw == 0 && (roleA || roleB)) {
      const unsigned elo = (unsigned)e1, ehi = (unsigned)(e1 >> 32);
      for (unsigned spins = 0; spins < SPIN_CAP; ++spins) {
        asm volatile("" ::: "memory");
        unsigned bad = 0;
#pragma unroll
        for (int i = 0; i < 4; ++i) {
          const u32x2 f = __builtin_amdgcn_raw_buffer_load_b64(rws, WS_FLAGS + (i * 64 + lane) * 8, 0, AUX_SC1);
          bad |= (f[0] ^ elo) | (f[1] ^ ehi);
        }
        if (__all(bad == 0)) break;
        __builtin_amdgcn_s_sleep(32);
      }
    }
    asm volatile("s_waitcnt vmcnt(0)" ::: "memory");
    wg_barrier();
  } else {
    const __amdgpu_buffer_rsrc_t rk = __builtin_amdgcn_make_buffer_rsrc((void*)(p.k + (size_t)h * SK * DH), 0, HEAD_BYTES, RSRC_W3);
    const __amdgpu_buffer_rsrc_t rv = __builtin_amdgcn_make_buffer_rsrc((void*)(p.v + (size_t)h * SK * DH), 0, HEAD_BYTES, RSRC_W3);
    const unsigned so0 = (unsigned)(s * NSW + w) * 4096u;
    u32x4 buf[16], pre1[16], pre2[16];
#define KSLOT(n) (*((n) < 16 || (n) >= 48 ? &buf[(n) & 15] : (n) < 32 ? &pre1[(n) & 15] : &pre2[(n) & 15]))
#define LD_KV(r, base, n) bld<AUX_NT>((r), vo + (unsigned)(((n) % 4) * 1024), (base) + (unsigned)((n) / 4) * STEP)
    __builtin_amdgcn_s_setprio(1);
    const unsigned t_start = (unsigned)__builtin_amdgcn_s_memrealtime();
#define IDLE(n) do { if ((n) / 16) asm volatile(".rept %0\n\ts_nop 15\n\t.endr" :: "n"((n) / 16) : "memory"); \
                     if ((n) % 16) asm volatile("s_nop %0" :: "n"((n) % 16 - 1) : "memory"); } while (0)
    wg_barrier();
#pragma unroll
    for (int j = 0; j < PRE; ++j) {
      if (j >= LIMP) asm volatile("s_waitcnt vmcnt(%0)" :: "n"(LIMP - 1) : "memory");
      KSLOT(j) = LD_KV(rk, so0, j);
      __builtin_amdgcn_sched_barrier(0);
    }
    wg_barrier();
    const u32x4 q = *(const LAS u32x4*)(L + L_QPK + 16 * c);

    LAS float* pw = (LAS float*)(L + L_P) + w * 512;
    float mrun = -INFINITY;
    const int jrev = ((lane & 8) >> 3) | ((lane & 4) >> 1) | ((lane & 2) << 1) | ((lane & 1) << 3);
    auto kgroup = [&](auto kind, int it, __amdgpu_buffer_rsrc_t rn, unsigned so_cur, unsigned so_nxt) {
      constexpr int KIND = decltype(kind)::value;
      float x[16];
#pragma unroll
      for (int j = 0; j < 16; ++j) {
        if (KIND < 3) {
          const int n = KIND * 16 + j;
#pragma unroll
          for (int m = k_issued(n - 1); m < k_issued(n); ++m) KSLOT(m) = LD_KV(rk, so0, m);
        } else {
          const int m = j + KD;
          IDLE(DLK);
          if (m < 16) buf[m] = LD_KV(rk, so_cur, m);
          else buf[m - 16] = LD_KV(rn, so_nxt, m - 16);
        }
        const u32x4 kv = KIND < 3 ? KSLOT(KIND * 16 + j) : buf[j];
        float d = dot2(kv[0], q[0], 0.f); d = dot2(kv[1], q[1], d); d = dot2(kv[2], q[2], d); d = dot2(kv[3], q[3], d);
        if (j & 1) {
          d = pair_reduce<XOR8, 8>(x[j - 1], d, lane);
          if ((j & 3) == 3) {
            d = pair_reduce<XOR7, 4>(x[j - 2], d, lane);
            if ((j & 7) == 7) {
              d = pair_reduce<XOR2, 2>(x[j - 4], d, lane);
              if (j == 15) d = pair_reduce<XOR1, 1>(x[j - 8], d, lane);
            }
          }
        }
        asm volatile("" : "+v"(d) :: "memory");
        x[j] = d;
        __builtin_amdgcn_sched_barrier(0);
      }
      const float sc = rbf(sum_x16(x[15]));
      mrun = fmaxf(mrun, sc);
      pw[half * 256 + it * 16 + jrev] = sc;
    };
    kgroup(std::integral_constant<int, 0>{}, 0, rk, 0u, 0u);
    kgroup(std::integral_constant<int, 1>{}, 1, rk, 0u, 0u);
    kgroup(std::integral_constant<int, 2>{}, 2, rk, 0u, 0u);
#pragma nounroll
    for (int it = 3; it < NG; ++it) {
      const bool lastg = it == NG - 1;
      kgroup(std::integral_constant<int, 3>{}, it, lastg ? rv : rk, so0 + it * 4 * STEP, lastg ? so0 : so0 + (it + 1) * 4 * STEP);
    }

    const float mw = wave_max(mrun);
    float lw = 0.f;
    lds_wait();
    {
      float sv[8];
#pragma unroll
      for (int g = 0; g < 8; ++g) sv[g] = pw[g * 64 + lane];
#pragma unroll
      for (int g = 0; g < 8; ++g) {
        const float pv = __builtin_amdgcn_exp2f((sv[g] - mw) * C2);
        lw += pv;
        ((LAS unsigned*)pw)[g * 64 + lane] = bfbits(pv);
      }
    }
    lw = wave_sum(lw);
    lds_wait();

    float acc[8];
#pragma unroll
    for (int k = 0; k < 8; ++k) acc[k] = 0.f;
    const LAS u32x4* pr = (const LAS u32x4*)((LAS unsigned*)pw + half * 256);
    u32x4 pp[4];
#pragma unroll
    for (int j4 = 0; j4 < 4; ++j4) pp[j4] = pr[j4];
    unsigned t_now = t_start;
    auto vgroup = [&](auto dist, auto paced, int it) {
      constexpr int DIST = decltype(dist)::value;
      constexpr bool PACED = decltype(paced)::value;
      const unsigned so_cur = so0 + it * 4 * STEP, so_nxt = it == NG - 1 ? HEAD_BYTES : so_cur + 4 * STEP;
      const int itn = it == NG - 1 ? it : it + 1;
#pragma unroll
      for (int j = 0; j < 16; ++j) {
        const int m = j + DIST;
        if (PACED) IDLE(DLV);
        if (m < 16) buf[m] = LD_KV(rv, so_cur, m);
        else buf[m - 16] = LD_KV(rv, so_nxt, m - 16);
        if (PACED && j == 8) t_now = (unsigned)__builtin_amdgcn_s_memrealtime();
        axpy8(acc, buf[j], pp[j >> 2][j & 3]);
        if ((j & 3) == 3) pp[j >> 2] = pr[itn * 4 + (j >> 2)];
        __builtin_amdgcn_sched_barrier(0);
      }
    };
    int it = 0;
#pragma nounroll
    do {
      vgroup(std::integral_constant<int, KD>{}, std::true_type{}, it);
      ++it;
    } while (it < NG && (int)(t_now - t_start) < TDRAIN);
    if (it < NG) {
#pragma unroll
      for (int m = KD; m < KD2; ++m) { buf[m] = LD_KV(rv, so0 + it * 4 * STEP, m); __builtin_amdgcn_sched_barrier(0); }
#pragma nounroll
      for (; it < NG; ++it) vgroup(std::integral_constant<int, KD2>{}, std::false_type{}, it);
    }

    f32x4 t;
#pragma unroll
    for (int k = 0; k < 4; ++k) {
      auto sw = __builtin_amdgcn_permlane32_swap(__float_as_uint(acc[k]), __float_as_uint(acc[k + 4]), false, false);
      t[k] = __uint_as_float(sw[0]) + __uint_as_float(sw[1]);
    }
    *(LAS f32x4*)(L + L_REC + w * 1024 + c * 32 + half * 16) = t;
    if (lane == 0) *(LAS u32x2*)(L + L_ML + w * 8) = u32x2{__float_as_uint(mw), __float_as_uint(lw)};
    __builtin_amdgcn_s_setprio(0);
    wg_barrier();
  }

  if (w == 0) {
    const u32x2 ml = *(const LAS u32x2*)(L + L_ML + (lane & 3) * 8);
    const f32x4 r0 = *(const LAS f32x4*)(L + L_REC + vo), r1 = *(const LAS f32x4*)(L + L_REC + 1024 + vo);
    const f32x4 r2 = *(const LAS f32x4*)(L + L_REC + 2048 + vo), r3 = *(const LAS f32x4*)(L + L_REC + 3072 + vo);
    const float mi = __uint_as_float(ml[0]);
    float M = fmaxf(mi, dpp<XOR1>(mi));
    M = fmaxf(M, dpp<XOR2>(M));
    const float wt = __builtin_amdgcn_exp2f((mi - M) * C2);
    const f32x4 o = bcast_fma4x4(wt, r0, r1, r2, r3);
    float Ls = wt * __uint_as_float(ml[1]);
    Ls += dpp<XOR1>(Ls);
    Ls += dpp<XOR2>(Ls);
    __builtin_amdgcn_raw_buffer_store_b128(u32x4{canon(o[0]), canon(o[1]), canon(o[2]), canon(o[3])}, rws, WS_PO + bb * 1024 + vo, 0, AUX_SC1);
    if (lane == 0) __builtin_amdgcn_raw_buffer_store_b64(u32x2{canon(M), canon(Ls)}, rws, WS_PML + bb * 8, 0, AUX_SC1);
  }
  if (!roleA && !roleB) return;

  const int n16 = lane & 15, g4 = lane >> 4;
  if (roleA) {
    bf16x8 wf[8];
    if (w < 7) {
#pragma unroll
      for (int kk = 0; kk < 8; ++kk) wf[kk] = bfrag(L + L_W, ACOLS * 2, 32 * kk, 16 * w, lane);
    }
    if (w < 4) {
      unsigned ov[16];
      u32x2 ml;
      for (unsigned spins = 0;; ++spins) {
        ml = __builtin_amdgcn_raw_buffer_load_b64(rws, WS_PML + (h * NSPLIT + n16) * 8, 0, AUX_SC1);
#pragma unroll
        for (int i = 0; i < 16; ++i) ov[i] = ld_sc1(rws, WS_PO + tid * 4, (h * NSPLIT + i) * 1024);
        __builtin_amdgcn_sched_barrier(0);
        unsigned mx = umax3(ml[0], ml[1], ov[0]);
#pragma unroll
        for (int i = 1; i < 15; i += 2) mx = umax3(mx, ov[i], ov[i + 1]);
        mx = mx > ov[15] ? mx : ov[15];
        if (__all(mx != SENT) || spins > SPIN_CAP) break;
        __builtin_amdgcn_s_sleep(PSLEEP);
        asm volatile("" ::: "memory");
      }
      const float mi = __uint_as_float(ml[0]);
      const float M = row_max(mi);
      const float wt = __builtin_amdgcn_exp2f((mi - M) * C2);
      const float rl = __builtin_amdgcn_rcpf(row_sum(wt * __uint_as_float(ml[1])));
      float r[16];
#pragma unroll
      for (int i = 0; i < 16; ++i) r[i] = __uint_as_float(ov[i]);
      ((LAS u16*)(L + L_OB))[tid] = (u16)bfbits(bcast_fma16(wt, r) * rl);
    }
    wg_barrier();
    if (w < 7) {
      bf16x8 af[8];
#pragma unroll
      for (int kk = 0; kk < 8; ++kk) af[kk] = *(const LAS bf16x8*)(L + L_OB + kk * 64 + g4 * 16);
      __builtin_amdgcn_sched_barrier(0);
      f32x4 d0 = {0.f, 0.f, 0.f, 0.f}, d1 = {0.f, 0.f, 0.f, 0.f};
#pragma unroll
      for (int kk = 0; kk < 8; kk += 2) { d0 = mfma(af[kk], wf[kk], d0); d1 = mfma(af[kk + 1], wf[kk + 1], d1); }
      if (lane < 16) st_sc1(rws, canon(d0[0] + d1[0]), WS_Y + (h * DM + s * ACOLS + 16 * w + lane) * 4);
    }
    return;
  }

  bf16x8 wf[14];
  float cs = 0.f;
  unsigned b1v = 0, b2v = 0, xv = 0;
  if (w < 4) {
    b1v = p.b1[jB * BCOLS + 16 * w + n16];
    b2v = p.b2[16 * jB + n16];
#pragma unroll
    for (int kk = 0; kk < 14; ++kk) wf[kk] = bfrag(L + L_W, BCOLS * 2, 32 * kk, 16 * w, lane);
    const u32x4 ones4 = {0x3F803F80u, 0x3F803F80u, 0x3F803F80u, 0x3F803F80u};
    f32x4 d = {0.f, 0.f, 0.f, 0.f};
#pragma unroll
    for (int kk = 0; kk < 14; ++kk) d = mfma(__builtin_bit_cast(bf16x8, ones4), wf[kk], d);
    cs = d[0];
  } else {
#pragma unroll
    for (int t = 0; t < 7; ++t) {
      wf[2 * t] = bfrag(L + L_W2, DM * 2, 0, ACOLS * (w - 4) + 16 * t, lane);
      wf[2 * t + 1] = bfrag(L + L_W2, DM * 2, 32, ACOLS * (w - 4) + 16 * t, lane);
    }
  }
  if (w < 7) {
    xv = p.x[tid];
    asm volatile("" : "+v"(xv), "+v"(b1v), "+v"(b2v), "+v"(cs));
    float xf = bf2f(xv);
    asm volatile("" : "+v"(xf));
    unsigned yv[16];
    for (unsigned spins = 0;; ++spins) {
#pragma unroll
      for (int i = 0; i < 16; ++i) yv[i] = ld_sc1(rws, WS_Y + tid * 4, i * DM * 4);
      __builtin_amdgcn_sched_barrier(0);
      unsigned mx = yv[0];
#pragma unroll
      for (int i = 1; i < 15; i += 2) mx = umax3(mx, yv[i], yv[i + 1]);
      mx = mx > yv[15] ? mx : yv[15];
      if (__all(mx != SENT) || spins > SPIN_CAP) break;
      __builtin_amdgcn_s_sleep(PSLEEP);
      asm volatile("" ::: "memory");
    }
    float a[8];
#pragma unroll
    for (int i = 0; i < 8; ++i) a[i] = __uint_as_float(yv[2 * i]) + __uint_as_float(yv[2 * i + 1]);
    const float at = ((a[0] + a[1]) + (a[2] + a[3])) + ((a[4] + a[5]) + (a[6] + a[7]));
    ((LAS u16*)(L + L_OB))[tid] = (u16)bfbits(xf + rbf(at));
  }
  wg_barrier();
  if (w < 4) {
    const u32x4 hr = *(const LAS u32x4*)(L + L_OB + (lane < 56 ? lane : 0) * 16);
    const unsigned h0 = ((const LAS u16*)(L + L_OB))[0];
    bf16x8 af[14];
#pragma unroll
    for (int kk = 0; kk < 14; ++kk) af[kk] = *(const LAS bf16x8*)(L + L_OB + kk * 64 + g4 * 16);
    __builtin_amdgcn_sched_barrier(0);
    f32x4 d0 = {0.f, 0.f, 0.f, 0.f}, d1 = {0.f, 0.f, 0.f, 0.f};
#pragma unroll
    for (int kk = 0; kk < 14; kk += 2) { d0 = mfma(af[kk], wf[kk], d0); d1 = mfma(af[kk + 1], wf[kk + 1], d1); }
    float mb, rden;
    ln_stats(hr, bf2f(h0), lane < 56, mb, rden);
#pragma unroll
    for (int kk = 0; kk < 14; ++kk) { __builtin_amdgcn_sched_group_barrier(0x002, 5, 0); __builtin_amdgcn_sched_group_barrier(0x008, 1, 0); }
    float a = rbf(fmaf(-mb, cs, d0[0] + d1[0]) * rden);
    a = rbf(a + bf2f(b1v));
    a = a > 0.f ? a : 0.f;
    if (lane < 16) ((LAS u16*)(L + L_F))[16 * w + lane] = (u16)bfbits(a * a);
  }
  wg_barrier();
  if (w >= 4) {
    const bf16x8 f0 = *(const LAS bf16x8*)(L + L_F + g4 * 16), f1 = *(const LAS bf16x8*)(L + L_F + 64 + g4 * 16);
    f32x4 d[7];
#pragma unroll
    for (int t = 0; t < 7; ++t) d[t] = mfma(f0, wf[2 * t], f32x4{0.f, 0.f, 0.f, 0.f});
    __builtin_amdgcn_sched_barrier(0);
#pragma unroll
    for (int t = 0; t < 7; ++t) d[t] = mfma(f1, wf[2 * t + 1], d[t]);
    float o[7];
#pragma unroll
    for (int t = 0; t < 7; ++t) o[t] = d[t][0];
    asm volatile("" : "+v"(o[0]), "+v"(o[1]), "+v"(o[2]), "+v"(o[3]), "+v"(o[4]), "+v"(o[5]), "+v"(o[6]));
    const bool g1 = lane & 16, g2 = lane & 32;
    const float t01 = g1 ? o[1] : o[0], t23 = g1 ? o[3] : o[2], t45 = g1 ? o[5] : o[4];
    const float v0 = g2 ? t23 : t01, v1 = g2 ? o[6] : t45;
    const unsigned off = WS_OP + (jB * DM + ACOLS * (w - 4) + lane) * 4;
    st_sc1(rws, canon(v0), off);
    if (lane < 48) st_sc1(rws, canon(v1), off + 256);
    return;
  }

  {
    const int col = 16 * jB + n16;
    const float hv = bf2f(((const LAS u16*)(L + L_OB))[col]);
    const float b2f = bf2f(b2v);
    const u32x2 e1v = *(const LAS u32x2*)(L + L_E);
    const unsigned off0 = WS_OP + ((tid >> 4) * DM + col) * 4;
    const unsigned off1 = (tid >> 4) + 16 < NB ? off0 + 16 * DM * 4 : WS_END;
    unsigned o0, o1;
    for (unsigned spins = 0;; ++spins) {
      o0 = ld_sc1(rws, off0, 0);
      o1 = ld_sc1(rws, off1, 0);
      __builtin_amdgcn_sched_barrier(0);
      if (__all((o0 > o1 ? o0 : o1) != SENT) || spins > SPIN_CAP) break;
      asm volatile("" ::: "memory");
    }
    const float a = sum_x32(sum_x16(__uint_as_float(o0) + __uint_as_float(o1)));
    if (lane < 16) ((LAS float*)(L + L_FIN))[w * 16 + lane] = a;
    wg_barrier();
    if (w == 0) {
      const LAS float* fp = (const LAS float*)(L + L_FIN) + n16;
      float t = (fp[0] + fp[16]) + (fp[32] + fp[48]);
      t = rbf(rbf(t) + b2f);
      if (lane < 16) p.out[col] = (u16)bfbits(hv + t);
      if (jB == 0 && lane == 0) __builtin_amdgcn_raw_buffer_store_b64(e1v, rws, WS_EPOCH, 0, AUX_SC1);
    }
  }
}

}

extern "C" void kernel_launch(void* const* d_in, const int* in_sizes, int n_in,
                              void* d_out, int out_size, void* d_ws, size_t ws_size,
                              hipStream_t stream) {
  Params p;
  p.x = (const u16*)d_in[0]; p.k = (const u16*)d_in[1]; p.v = (const u16*)d_in[2];
  p.wq = (const u16*)d_in[3]; p.wo = (const u16*)d_in[4];
  p.w1 = (const u16*)d_in[6]; p.b1 = (const u16*)d_in[7]; p.w2 = (const u16*)d_in[8]; p.b2 = (const u16*)d_in[9];
  p.out = (u16*)d_out; p.ws = (unsigned char*)d_ws;
  (void)hipFuncSetAttribute((const void*)fused_decode_block, hipFuncAttributeMaxDynamicSharedMemorySize, L_BYTES);
  fused_decode_block<<<NWG, NTHR, L_BYTES, stream>>>(p);
}
```
